```python
import torch

def _softmax(x, axis=-1):
    return torch.softmax(x, dim=axis)

def _gelu(x):
    return torch.nn.functional.gelu(x, approximate='tanh')

def _silu(x):
    return torch.nn.functional.silu(x)

def _sigmoid(x):
    return torch.sigmoid(x)

def _layernorm(x, axis=-1, eps=1e-05):
    m = x.mean(dim=axis, keepdim=True)
    v = x.var(dim=axis, keepdim=True, unbiased=False)
    return (x - m) / torch.sqrt(v + eps)

def _scatter_add(tgt, idx, src, axis=0):
    return tgt.clone().index_add_(axis, idx, src)

def _segment_sum(src, seg, n):
    out = torch.zeros((n,) + src.shape[1:], dtype=src.dtype)
    return out.index_add_(0, seg, src)

def _pad(x, pw):
    flat = tuple((v for pair in reversed(pw) for v in pair))
    return torch.nn.functional.pad(x, flat)
kernel_name = 'standalone__kv_cache_quant_pre_rope_vs_post_rope__sampling'

def setup_inputs():

    _w = lambda a: (lambda t: t.to(torch.bfloat16) if t.is_floating_point() else t)(torch.as_tensor(a))
    B, Sq, Skv = (8, 1, 8192)
    H, Dh0 = (8, 128)
    G = -1 if -1 > 0 else Dh0
    G = max(2, G // 2 * 2)
    Dh = max(G, Dh0 // G * G)
    Skv = max(Skv, Sq)
    ng = Dh // G
    bits = 4 if 4 > 1 else 4
    bits = max(2, min(8, bits))
    qmax = (1 << bits) - 1
    half = Dh // 2
    inv = 1.0 / 10000.0 ** (torch.arange(half) / max(1, half))
    ang_k = torch.arange(Skv)[:, None] * inv[None, :]
    ang_q = torch.arange(Skv - Sq, Skv)[:, None] * inv[None, :]
    kc = torch.cat([torch.cos(ang_k), torch.cos(ang_k)], dim=-1)
    ks = torch.cat([torch.sin(ang_k), torch.sin(ang_k)], dim=-1)
    qc = torch.cat([torch.cos(ang_q), torch.cos(ang_q)], dim=-1)
    qs = torch.cat([torch.sin(ang_q), torch.sin(ang_q)], dim=-1)
    qk_i = torch.randint(0, qmax + 1, (B, H, Skv, Dh)).to(torch.int32)
    qv_i = torch.randint(0, qmax + 1, (B, H, Skv, Dh)).to(torch.int32)
    ksc = torch.abs((lambda *s: torch.randn(*s, dtype=torch.bfloat16) * 0.2)(B, H, Skv, ng)) * 0.1 + 0.01
    kzp = torch.full((B, H, Skv, ng), qmax / 2.0)
    vsc = torch.abs((lambda *s: torch.randn(*s, dtype=torch.bfloat16) * 0.2)(B, H, Skv, ng)) * 0.1 + 0.01
    vzp = torch.full((B, H, Skv, ng), qmax / 2.0)
    return {'q': _w((lambda *s: torch.randn(*s, dtype=torch.bfloat16) * 6.4)(B, H, Sq, Dh) * 0.5), 'qk': _w(qk_i), 'k_scales': _w(ksc), 'k_zeros': _w(kzp), 'qv': _w(qv_i), 'v_scales': _w(vsc), 'v_zeros': _w(vzp), 'q_cos': _w(qc), 'q_sin': _w(qs), 'k_cos': _w(kc), 'k_sin': _w(ks)}

def reference(q, qk, k_scales, k_zeros, qv, v_scales, v_zeros, q_cos, q_sin, k_cos, k_sin):
    B, H, Sq, Dh = q.shape
    ng = k_scales.shape[-1]
    G = Dh // ng
    half = Dh // 2
    ks = (lambda x, n, axis: torch.repeat_interleave(x, n, dim=axis))(k_scales, G, axis=-1)
    kz = (lambda x, n, axis: torch.repeat_interleave(x, n, dim=axis))(k_zeros, G, axis=-1)
    k = ((lambda x, dt: x.to(dt))(qk, q.dtype) - kz) * ks
    vs = (lambda x, n, axis: torch.repeat_interleave(x, n, dim=axis))(v_scales, G, axis=-1)
    vz = (lambda x, n, axis: torch.repeat_interleave(x, n, dim=axis))(v_zeros, G, axis=-1)
    v = ((lambda x, dt: x.to(dt))(qv, q.dtype) - vz) * vs
    k1 = k[..., :half].clone()
    k2 = k[..., half:].clone()
    krh = torch.concatenate([-k2, k1], axis=-1)
    kr = k * k_cos[None, None, :, :] + krh * k_sin[None, None, :, :]
    q1 = q[..., :half].clone()
    q2 = q[..., half:].clone()
    qrh = torch.concatenate([-q2, q1], axis=-1)
    qr = q * q_cos[None, None, :, :] + qrh * q_sin[None, None, :, :]
    logits = torch.einsum('bhqd,bhkd->bhqk', qr, kr) * float(Dh) ** (-0.5)
    attn = _softmax(logits, axis=-1)
    out = torch.einsum('bhqk,bhkd->bhqd', attn, v)
    return out
```

```cpp
#include <hip/hip_runtime.h>
#include <hip/hip_bf16.h>

namespace {

using u32x4 = __attribute__((ext_vector_type(4))) unsigned;
using u32x2 = __attribute__((ext_vector_type(2))) unsigned;
using i32x4 = __attribute__((ext_vector_type(4))) int;
using f32x4 = __attribute__((ext_vector_type(4))) float;
using f32x2 = __attribute__((ext_vector_type(2))) float;
typedef __amdgpu_buffer_rsrc_t rsrc_t;

constexpr int NH = 64;
constexpr int SKV = 8192;
constexpr int DH = 128;
constexpr int NSPLIT = 4;
constexpr int WAVES = 4;
constexpr int KEYS_WG = SKV / NSPLIT;
constexpr int TILES_W = KEYS_WG / 4 / WAVES;
#define RING 4
constexpr int NPART = NSPLIT * WAVES;
constexpr int PART_F = 132;
constexpr unsigned KV_BYTES = (unsigned)NH * SKV * DH * 4u;
constexpr unsigned CS_BYTES = (unsigned)SKV * DH * 2u;
constexpr unsigned SC_BYTES = (unsigned)NH * SKV * 2u;
constexpr float LOGIT_SCALE = 0.08838834764831845f * 1.4426950408889634f;

__device__ __forceinline__ rsrc_t make_rsrc(const void* p, unsigned bytes) {
  return __builtin_amdgcn_make_buffer_rsrc(const_cast<void*>(p), 0, bytes, 0x00020000);
}
template <int AUX>
__device__ __forceinline__ u32x4 ld16(rsrc_t r, unsigned voff, unsigned soff) {
  return __builtin_bit_cast(u32x4, __builtin_amdgcn_raw_buffer_load_b128(r, voff, soff, AUX));
}
template <int AUX>
__device__ __forceinline__ u32x2 ld8(rsrc_t r, unsigned voff, unsigned soff) {
  return __builtin_bit_cast(u32x2, __builtin_amdgcn_raw_buffer_load_b64(r, voff, soff, AUX));
}
__device__ __forceinline__ float bf_lo(unsigned x) { return __builtin_bit_cast(float, x << 16); }
__device__ __forceinline__ float bf_hi(unsigned x) { return __builtin_bit_cast(float, x & 0xffff0000u); }
__device__ __forceinline__ f32x4 unpack4(u32x2 x) { return f32x4{bf_lo(x[0]), bf_hi(x[0]), bf_lo(x[1]), bf_hi(x[1])}; }
__device__ __forceinline__ float rbf(float x) { return (float)(__bf16)x; }
__device__ __forceinline__ f32x4 rbf4(f32x4 x) { return f32x4{rbf(x[0]), rbf(x[1]), rbf(x[2]), rbf(x[3])}; }
__device__ __forceinline__ f32x4 cvt4(u32x4 x) { return __builtin_convertvector(__builtin_bit_cast(i32x4, x), f32x4); }

template <int CTRL>
__device__ __forceinline__ float dpp(float x) {
  return __builtin_bit_cast(float, __builtin_amdgcn_mov_dpp(__builtin_bit_cast(int, x), CTRL, 0xf, 0xf, true));
}
__device__ __forceinline__ float row_sum(float x) {
  x += dpp<0xB1>(x);
  x += dpp<0x4E>(x);
  x += dpp<0x141>(x);
  x += dpp<0x140>(x);
  return x;
}
__device__ __forceinline__ float xrow_max(float x) {
  auto s = __builtin_amdgcn_permlane16_swap(__float_as_uint(x), __float_as_uint(x), false, false);
  x = fmaxf(__uint_as_float(s[0]), __uint_as_float(s[1]));
  auto t = __builtin_amdgcn_permlane32_swap(__float_as_uint(x), __float_as_uint(x), false, false);
  return fmaxf(__uint_as_float(t[0]), __uint_as_float(t[1]));
}
__device__ __forceinline__ float xrow_sum(float x) {
  auto s = __builtin_amdgcn_permlane16_swap(__float_as_uint(x), __float_as_uint(x), false, false);
  x = __uint_as_float(s[0]) + __uint_as_float(s[1]);
  auto t = __builtin_amdgcn_permlane32_swap(__float_as_uint(x), __float_as_uint(x), false, false);
  return __uint_as_float(t[0]) + __uint_as_float(t[1]);
}

struct Tile {
  u32x4 k0, k1, v0, v1;
  u32x2 c0, c1, s0, s1;
};

__global__ void __launch_bounds__(WAVES * 64)
attn_partial(const unsigned short* __restrict__ q, const int* __restrict__ qk,
             const unsigned short* __restrict__ ksc, const unsigned short* __restrict__ kzp,
             const int* __restrict__ qv, const unsigned short* __restrict__ vsc,
             const unsigned short* __restrict__ vzp, const unsigned short* __restrict__ qcos,
             const unsigned short* __restrict__ qsin, const unsigned short* __restrict__ kcos,
             const unsigned short* __restrict__ ksin, float* __restrict__ part) {
  __shared__ __attribute__((aligned(16))) float sc_lds[KEYS_WG * 4];
  const int lane = threadIdx.x & 63;
  const int wv = __builtin_amdgcn_readfirstlane(threadIdx.x >> 6);
  const int r = lane >> 4, c = lane & 15;
  const int split = blockIdx.x % NSPLIT, bh = blockIdx.x / NSPLIT;
  const unsigned key0 = split * KEYS_WG;

  const rsrc_t rk = make_rsrc(qk, KV_BYTES), rv = make_rsrc(qv, KV_BYTES);
  const rsrc_t rc = make_rsrc(kcos, CS_BYTES), rs = make_rsrc(ksin, CS_BYTES);

  const u32x2 q0r = *reinterpret_cast<const u32x2*>(q + bh * DH + 4 * c);
  const u32x2 q1r = *reinterpret_cast<const u32x2*>(q + bh * DH + 64 + 4 * c);
  const u32x2 qc0r = *reinterpret_cast<const u32x2*>(qcos + 4 * c);
  const u32x2 qc1r = *reinterpret_cast<const u32x2*>(qcos + 64 + 4 * c);
  const u32x2 qs0r = *reinterpret_cast<const u32x2*>(qsin + 4 * c);
  const u32x2 qs1r = *reinterpret_cast<const u32x2*>(qsin + 64 + 4 * c);

  const unsigned sc_off = (bh * SKV + key0) * 2u;
  const u32x4 rks = ld16<2>(make_rsrc(ksc, SC_BYTES), threadIdx.x * 16u, sc_off);
  const u32x4 rkz = ld16<2>(make_rsrc(kzp, SC_BYTES), threadIdx.x * 16u, sc_off);
  const u32x4 rvs = ld16<2>(make_rsrc(vsc, SC_BYTES), threadIdx.x * 16u, sc_off);
  const u32x4 rvz = ld16<2>(make_rsrc(vzp, SC_BYTES), threadIdx.x * 16u, sc_off);
  __builtin_amdgcn_sched_barrier(0);

  const unsigned kv_voff = r * 512u + c * 16u, cs_voff = r * 256u + c * 8u;
  const unsigned kv_base = (bh * SKV + key0 + 4 * wv) * 512u, cs_base = (key0 + 4 * wv) * 256u;
  auto issue = [&](Tile& t, unsigned i) {
    const unsigned ko = kv_base + i * (4u * WAVES * 512u), co = cs_base + i * (4u * WAVES * 256u);
    t.k0 = ld16<2>(rk, kv_voff, ko);
    t.k1 = ld16<2>(rk, kv_voff + 256u, ko);
    t.c0 = ld8<0>(rc, cs_voff, co);
    t.c1 = ld8<0>(rc, cs_voff + 128u, co);
    t.s0 = ld8<0>(rs, cs_voff, co);
    t.s1 = ld8<0>(rs, cs_voff + 128u, co);
    t.v0 = ld16<2>(rv, kv_voff, ko);
    t.v1 = ld16<2>(rv, kv_voff + 256u, ko);
  };
  Tile ring[RING];
#pragma unroll
  for (int i = 0; i < RING; ++i) {
    issue(ring[i], i);
    __builtin_amdgcn_sched_barrier(0);
  }

  const f32x4 q0 = unpack4(q0r), q1 = unpack4(q1r);
  const f32x4 qr0 = rbf4(rbf4(q0 * unpack4(qc0r)) + rbf4(-q1 * unpack4(qs0r)));
  const f32x4 qr1 = rbf4(rbf4(q1 * unpack4(qc1r)) + rbf4(q0 * unpack4(qs1r)));

#pragma unroll
  for (int j = 0; j < 4; ++j) {
    f32x4* dst = reinterpret_cast<f32x4*>(sc_lds) + threadIdx.x * 8 + 2 * j;
    dst[0] = f32x4{bf_lo(rks[j]) * LOGIT_SCALE, bf_lo(rkz[j]), bf_lo(rvs[j]), bf_lo(rvz[j])};
    dst[1] = f32x4{bf_hi(rks[j]) * LOGIT_SCALE, bf_hi(rkz[j]), bf_hi(rvs[j]), bf_hi(rvz[j])};
  }
  __syncthreads();

  float m = -INFINITY, l = 0.f;
  f32x4 a0 = {0.f, 0.f, 0.f, 0.f}, a1 = {0.f, 0.f, 0.f, 0.f};
  const f32x4* scp = reinterpret_cast<const f32x4*>(sc_lds) + 4 * wv + r;
  auto consume = [&](const Tile& t, unsigned i) {
    const f32x4 sc = scp[i * (4 * WAVES)];
    const f32x4 w0 = qr0 * unpack4(t.c0) + qr1 * unpack4(t.s1);
    const f32x4 w1 = qr1 * unpack4(t.c1) - qr0 * unpack4(t.s0);
    const f32x4 d = (cvt4(t.k0) - sc[1]) * w0 + (cvt4(t.k1) - sc[1]) * w1;
    const float s = row_sum((d[0] + d[1]) + (d[2] + d[3])) * sc[0];
    const float mn = fmaxf(m, s);
    const float alpha = __builtin_amdgcn_exp2f(m - mn);
    const float p = __builtin_amdgcn_exp2f(s - mn);
    m = mn;
    l = l * alpha + p;
    const float pv = p * sc[2];
    a0 = a0 * alpha + (cvt4(t.v0) - sc[3]) * pv;
    a1 = a1 * alpha + (cvt4(t.v1) - sc[3]) * pv;
  };

  for (unsigned i = 0; i < TILES_W - RING; i += RING) {
#pragma unroll
    for (int j = 0; j < RING; ++j) {
      consume(ring[j], i + j);
      __builtin_amdgcn_sched_barrier(0);
      issue(ring[j], i + j + RING);
      __builtin_amdgcn_sched_barrier(0);
    }
  }
#pragma unroll
  for (int j = 0; j < RING; ++j) consume(ring[j], TILES_W - RING + j);

  const float mw = xrow_max(m);
  const float f = __builtin_amdgcn_exp2f(m - mw);
  const float lw = xrow_sum(l * f);
  f32x4 o0, o1;
#pragma unroll
  for (int j = 0; j < 4; ++j) {
    o0[j] = xrow_sum(a0[j] * f);
    o1[j] = xrow_sum(a1[j] * f);
  }
  float* pp = part + (size_t)(bh * NPART + split * WAVES + wv) * PART_F;
  if (r == 0) *reinterpret_cast<f32x4*>(pp + 4 * c) = o0;
  if (r == 1) *reinterpret_cast<f32x4*>(pp + 64 + 4 * c) = o1;
  if (lane == 32) *reinterpret_cast<f32x2*>(pp + 128) = f32x2{mw, lw};
}

__global__ void __launch_bounds__(DH)
attn_combine(const float* __restrict__ part, __hip_bfloat16* __restrict__ out) {
  const int bh = blockIdx.x, d = threadIdx.x;
  const float* pp = part + (size_t)bh * NPART * PART_F;
  float mv[NPART], lv[NPART], ov[NPART];
#pragma unroll
  for (int i = 0; i < NPART; ++i) {
    mv[i] = pp[i * PART_F + 128];
    lv[i] = pp[i * PART_F + 129];
    ov[i] = pp[i * PART_F + d];
  }
  float M = mv[0];
#pragma unroll
  for (int i = 1; i < NPART; ++i) M = fmaxf(M, mv[i]);
  float L = 0.f, O = 0.f;
#pragma unroll
  for (int i = 0; i < NPART; ++i) {
    const float w = __builtin_amdgcn_exp2f(mv[i] - M);
    L += w * lv[i];
    O += w * ov[i];
  }
  out[bh * DH + d] = __float2bfloat16(O / L);
}

}

extern "C" void kernel_launch(void* const* d_in, const int* in_sizes, int n_in, void* d_out, int out_size,
                              void* d_ws, size_t ws_size, hipStream_t stream) {
  using us = const unsigned short*;
  float* part = static_cast<float*>(d_ws);
  attn_partial<<<NH * NSPLIT, WAVES * 64, 0, stream>>>(
      (us)d_in[0], (const int*)d_in[1], (us)d_in[2], (us)d_in[3], (const int*)d_in[4], (us)d_in[5], (us)d_in[6],
      (us)d_in[7], (us)d_in[8], (us)d_in[9], (us)d_in[10], part);
  attn_combine<<<NH, DH, 0, stream>>>(part, static_cast<__hip_bfloat16*>(d_out));
}
```

```cpp
#define CODEPF 16
#define EARLY_ORG 0
#define SYNC_MEAN 1
#define SYNC_AT 280
#define LAT0 20
#define PACE_K 6850
#define PACE_V 6850
#include <hip/hip_runtime.h>
#include <hip/hip_bf16.h>

#define PACE_P PACE_K
#define HT0 500
#define HDT 400
#define SYNC_ADD 0
#define JUMP 0
#define TOUCH 1
#define DIAG 0

namespace {

using u32x4 = __attribute__((ext_vector_type(4))) unsigned;
using u32x2 = __attribute__((ext_vector_type(2))) unsigned;
using i32x4 = __attribute__((ext_vector_type(4))) int;
using f32x4 = __attribute__((ext_vector_type(4))) float;
using f32x2 = __attribute__((ext_vector_type(2))) float;
using u64 = unsigned long long;
typedef __amdgpu_buffer_rsrc_t rsrc_t;
typedef __attribute__((address_space(1))) u64 gu64;
typedef __attribute__((address_space(3))) unsigned lu32;
#define RLX_AGENT __ATOMIC_RELAXED, __HIP_MEMORY_SCOPE_AGENT
#define RLX_WG __ATOMIC_RELAXED, __HIP_MEMORY_SCOPE_WORKGROUP

constexpr int NH = 64;
constexpr int SKV = 8192;
constexpr int DH = 128;
constexpr int WAVES = 4;
constexpr int HG = 4;
constexpr int NSLICE = 16;
constexpr int KEYS = SKV / NSLICE;
constexpr int STEPS = KEYS * HG / 4 / WAVES;
constexpr unsigned KV_BYTES = (unsigned)NH * SKV * DH * 4u;
constexpr unsigned CS_BYTES = (unsigned)SKV * DH * 2u;
constexpr unsigned SC_BYTES = (unsigned)NH * SKV * 2u;
constexpr float LOGIT_SCALE = 0.08838834764831845f * 1.4426950408889634f;

constexpr int W_O = 0;
constexpr int W_ML = HG * NSLICE * 64;
constexpr int W_ACK = W_ML + HG * NSLICE;
constexpr int W_BEAT = W_ACK + 32;
constexpr int W_TIME = W_BEAT + 16;
constexpr int W_GROUP = W_ML + 128;
constexpr u64 POISON = ~0ull;
constexpr unsigned SPIN_CAP = 1u << 18;

__device__ __forceinline__ rsrc_t make_rsrc(const void* p, unsigned bytes) {
  return __builtin_amdgcn_make_buffer_rsrc(const_cast<void*>(p), 0, bytes, 0x00020000);
}
template <int AUX>
__device__ __forceinline__ u32x4 ld16(rsrc_t r, unsigned voff, unsigned soff) {
  return __builtin_bit_cast(u32x4, __builtin_amdgcn_raw_buffer_load_b128(r, voff, soff, AUX));
}
template <int AUX>
__device__ __forceinline__ u32x2 ld8(rsrc_t r, unsigned voff, unsigned soff) {
  return __builtin_bit_cast(u32x2, __builtin_amdgcn_raw_buffer_load_b64(r, voff, soff, AUX));
}
__device__ __forceinline__ float bf_lo(unsigned x) { return __builtin_bit_cast(float, x << 16); }
__device__ __forceinline__ float bf_hi(unsigned x) { return __builtin_bit_cast(float, x & 0xffff0000u); }
__device__ __forceinline__ f32x4 unpack4(u32x2 x) { return f32x4{bf_lo(x[0]), bf_hi(x[0]), bf_lo(x[1]), bf_hi(x[1])}; }
__device__ __forceinline__ float rbf(float x) { return (float)(__bf16)x; }
__device__ __forceinline__ f32x4 rbf4(f32x4 x) { return f32x4{rbf(x[0]), rbf(x[1]), rbf(x[2]), rbf(x[3])}; }
__device__ __forceinline__ f32x4 cvt4(u32x4 x) { return __builtin_convertvector(__builtin_bit_cast(i32x4, x), f32x4); }

template <int CTRL>
__device__ __forceinline__ float dpp(float x) {
  return __builtin_bit_cast(float, __builtin_amdgcn_mov_dpp(__builtin_bit_cast(int, x), CTRL, 0xf, 0xf, true));
}
__device__ __forceinline__ float row_sum(float x) {
  x += dpp<0xB1>(x);
  x += dpp<0x4E>(x);
  x += dpp<0x141>(x);
  x += dpp<0x140>(x);
  return x;
}
__device__ __forceinline__ float row_max(float x) {
  x = fmaxf(x, dpp<0xB1>(x));
  x = fmaxf(x, dpp<0x4E>(x));
  x = fmaxf(x, dpp<0x141>(x));
  x = fmaxf(x, dpp<0x140>(x));
  return x;
}
__device__ __forceinline__ float fold32(float a, float b) {
  auto t = __builtin_amdgcn_permlane32_swap(__float_as_uint(a), __float_as_uint(b), false, false);
  return __uint_as_float(t[0]) + __uint_as_float(t[1]);
}
__device__ __forceinline__ float fold16(float a, float b) {
  auto t = __builtin_amdgcn_permlane16_swap(__float_as_uint(a), __float_as_uint(b), false, false);
  return __uint_as_float(t[0]) + __uint_as_float(t[1]);
}
__device__ __forceinline__ float xrow_max(float x) {
  auto s = __builtin_amdgcn_permlane16_swap(__float_as_uint(x), __float_as_uint(x), false, false);
  x = fmaxf(__uint_as_float(s[0]), __uint_as_float(s[1]));
  auto t = __builtin_amdgcn_permlane32_swap(__float_as_uint(x), __float_as_uint(x), false, false);
  return fmaxf(__uint_as_float(t[0]), __uint_as_float(t[1]));
}
__device__ __forceinline__ u64 pack_pair(float a, float b) {
  const u64 x = (u64)__float_as_uint(a) | ((u64)__float_as_uint(b) << 32);
  return x == POISON ? 0x7fc000007fc00000ull : x;
}

__device__ __forceinline__ constexpr unsigned global_key(unsigned local) { return (local & 63) | ((local >> 6) << 8); }
__device__ __forceinline__ constexpr unsigned ktile_key(unsigned t) {
  return ((t & 3) << 6) | ((t & 4) << 1) | ((t & 8) << 2) | ((t & 16) << 4);
}
__device__ __forceinline__ constexpr unsigned vstep_key(unsigned i) {
  return ((i & 1) << 4) | ((i & 6) << 1) | ((i >> 3) << 5);
}

__device__ __forceinline__ int wait_until(unsigned target) {
  int late;
  asm volatile("pace_%=:\n\ts_memrealtime vcc\n\ts_waitcnt lgkmcnt(0)\n\ts_sub_u32 %0, vcc_lo, %1\n\t"
               "s_cmp_gt_u32 %0, 0xfffff000\n\ts_cbranch_scc1 pace_%=" : "=&s"(late) : "s"(target) : "vcc", "scc");
  return late;
}
__device__ __forceinline__ void sleep_until(unsigned target) {
  asm volatile("nap_%=:\n\ts_sleep 4\n\ts_memrealtime vcc\n\ts_waitcnt lgkmcnt(0)\n\ts_sub_u32 vcc_lo, vcc_lo, %0\n\t"
               "s_cmp_gt_u32 vcc_lo, 0xfffff000\n\ts_cbranch_scc1 nap_%=" ::"s"(target) : "vcc", "scc", "memory");
}

template <int SLEEP>
__device__ __forceinline__ void wait_flag(lu32* f) {
#pragma nounroll
  for (unsigned spins = 0; __builtin_amdgcn_readfirstlane(__hip_atomic_load(f, RLX_WG)) == 0 && spins < SPIN_CAP; ++spins)
    __builtin_amdgcn_s_sleep(SLEEP);
}
__device__ __forceinline__ void set_flag(lu32* f) { __hip_atomic_store(f, 1u, RLX_WG); }

struct Tile { u32x4 lo, hi; };
struct Rope { u32x2 c0, c1, s0, s1; };

#define STAMP(k)

constexpr int RING = 8;

__global__ void __launch_bounds__((WAVES + 1) * 64)
attn_fused(const unsigned short* __restrict__ q, const int* __restrict__ qk,
           const unsigned short* __restrict__ ksc, const unsigned short* __restrict__ kzp,
           const int* __restrict__ qv, const unsigned short* __restrict__ vsc,
           const unsigned short* __restrict__ vzp, const unsigned short* __restrict__ qcos,
           const unsigned short* __restrict__ qsin, const unsigned short* __restrict__ kcos,
           const unsigned short* __restrict__ ksin, u64* __restrict__ ws, unsigned* __restrict__ out) {
  __shared__ __attribute__((aligned(16))) f32x2 lds[HG * KEYS + WAVES * HG * DH / 2 + 4];
  lu32* done = (lu32*)(lds + HG * KEYS + WAVES * HG * DH / 2);
  lu32* org_w = done + 1;
  lu32* eff_w = done + 4;
  const int lane = threadIdx.x & 63;
  const int wv = __builtin_amdgcn_readfirstlane(threadIdx.x >> 6);
  const unsigned b = blockIdx.x;
  const unsigned slice = b & 15;
  const unsigned key0 = ((b & 3) << 6) | ((b & 4) << 10) | ((b & 8) << 8);
  const unsigned head0 = ((b >> 7) & 1) | ((b >> 5) & 2) | ((b >> 2) & 12) | ((b >> 3) & 16);
  auto head_of = [&](unsigned u) { return head0 ^ (((u & 1) << 5) | ((u & 2) << 3)); };
  unsigned warm0 = 0, warm1 = 0;
  if (wv == WAVES) {
    typedef __attribute__((address_space(1))) const char* gptr;
    const int arg = lane < 4 ? 1 : lane < 8 ? 4 : min(lane - 8, 12);
    const gptr p = ((const __attribute__((address_space(4))) gptr*)__builtin_amdgcn_kernarg_segment_ptr())[arg];
    u64 pc;
    asm volatile("s_getpc_b64 %0" : "=s"(pc));
    warm0 = __builtin_amdgcn_raw_buffer_load_b32(make_rsrc((const void*)(pc & ~255ull), CODEPF * 1024), lane * 16u + (b >> 3) * 1024u, 0, 0);
    const unsigned off = lane < 8 ? (head_of(lane & 3) * SKV + key0) * 512u
                       : arg == 11 ? (1u << 20) : arg == 12 ? head_of(slice & 3) * 256u : 0u;
    if (TOUCH) warm1 = *(const __attribute__((address_space(1))) unsigned*)(p + off);
    asm volatile("" ::"v"(warm0), "v"(warm1));
    __hip_atomic_store(org_w + !EARLY_ORG, (unsigned)__builtin_amdgcn_s_memrealtime() - LAT0, RLX_WG);
  }
  const int r = lane >> 4, c = lane & 15;
  const unsigned wkey = ((wv & 1) << 4) | ((wv & 2) << 1);
  const unsigned kv_voff = r * 512u + c * 16u;
  const unsigned first = (head0 * SKV + key0 + wkey) * 512u;
  asm volatile("" ::"s"(first), "v"(kv_voff));
  __builtin_amdgcn_sched_barrier(0);
  asm volatile("" ::"s"(q), "s"(qk), "s"(ksc), "s"(kzp), "s"(qv), "s"(vsc), "s"(vzp), "s"(qcos), "s"(qsin), "s"(kcos),
               "s"(ksin), "s"(ws), "s"(out));
  __builtin_amdgcn_sched_barrier(0);
  gu64* area = (gu64*)ws + (size_t)(b >> 4) * W_GROUP;

  if (wv == WAVES) {
    const unsigned quarter = slice >> 2;
    gu64* src = area + W_O + (slice & 3) * (NSLICE * 64) + r * 64 + quarter * 16 + c;
    unsigned* dst = out + head_of(slice & 3) * (DH / 2) + (quarter >> 1) * 32 + c * 2 + (quarter & 1);
    __hip_atomic_store(done, 0u, RLX_WG);
    unsigned org = __builtin_amdgcn_readfirstlane(__hip_atomic_load(org_w + !EARLY_ORG, RLX_WG));
    const unsigned first_issue = __builtin_amdgcn_readfirstlane(__hip_atomic_load(eff_w, RLX_WG));
    org = (first_issue - org < 200u) ? first_issue : org;
    if (lane == 0) __hip_atomic_store(area + W_TIME + slice, (u64)org, RLX_AGENT);
    const u64 entry_beat = __hip_atomic_load(area + W_BEAT, RLX_AGENT);
    sleep_until(EARLY_ORG ? org + SYNC_AT : first_issue + SYNC_AT);
    {
      const int d = (int)((unsigned)__hip_atomic_load(area + W_TIME + c, RLX_AGENT) - org);
      const bool ok = d > -300 && d < 300;
      int shift = (SYNC_MEAN ? (int)(row_sum(ok ? (float)d : 0.f) / row_sum(ok ? 1.f : 0.f)) : (int)row_max(ok ? (float)d : 0.f)) + SYNC_ADD;
      asm volatile("" : "+v"(shift));
      org += __builtin_amdgcn_readfirstlane(shift);
      __hip_atomic_store(org_w, org, RLX_WG);
    }
    sleep_until(org + HT0);
    unsigned entry[4];
#pragma unroll
    for (int k = 0; k < 4; ++k) entry[k] = (unsigned)__hip_atomic_load(src + k * 256, RLX_AGENT);
    u64 beat = entry_beat;
    if (slice == 0) {
      __hip_atomic_store(area + W_ML + lane, POISON, RLX_AGENT);
      if (lane < NSLICE) __hip_atomic_store(area + W_ACK + lane, POISON, RLX_AGENT);
      asm volatile("s_waitcnt vmcnt(0)" ::: "memory");
      if (lane == 0) __hip_atomic_store(area + W_BEAT, ++beat, RLX_AGENT);
    }
    asm volatile("s_waitcnt vmcnt(0)" : "+v"(entry[0]), "+v"(entry[1]), "+v"(entry[2]), "+v"(entry[3])::"memory");
    sleep_until(org + HT0 + HDT);
    unsigned spins = 0;
#pragma nounroll
    while (__all(__hip_atomic_load(area + W_BEAT, RLX_AGENT) == entry_beat) && ++spins < SPIN_CAP)
      __builtin_amdgcn_s_sleep(64);
    if (lane == 0) __hip_atomic_store(area + W_ACK + slice, 0ull, RLX_AGENT);
    sleep_until(org + HT0 + 2 * HDT);
#pragma nounroll
    while (!__all(__hip_atomic_load(area + W_ACK + c, RLX_AGENT) != POISON) && ++spins < SPIN_CAP) {
      if (slice == 0 && lane == 0) __hip_atomic_store(area + W_BEAT, ++beat, RLX_AGENT);
      __builtin_amdgcn_s_sleep(64);
    }
    STAMP(1);
    asm volatile("s_waitcnt lgkmcnt(0)" ::: "memory");
    __builtin_amdgcn_s_barrier();
    sleep_until((unsigned)__builtin_amdgcn_s_memrealtime() + 2 * HDT);
    u64 ml;
#pragma nounroll
    for (;;) {
      ml = __hip_atomic_load(area + W_ML + (slice & 3) * NSLICE + c, RLX_AGENT);
      if (__all(ml != POISON) || ++spins >= SPIN_CAP) break;
      __builtin_amdgcn_s_sleep(64);
    }
    const float mk = __uint_as_float((unsigned)ml), lk = __uint_as_float((unsigned)(ml >> 32));
    const float e = __builtin_amdgcn_exp2f(mk - row_max(mk));
    const float wt = e / row_sum(e * lk);
    float wk[4];
#pragma unroll
    for (int k = 0; k < 4; ++k) wk[k] = __shfl(wt, 4 * k + r);
    asm volatile("" : "+v"(wk[0]), "+v"(wk[1]), "+v"(wk[2]), "+v"(wk[3]), "+v"(src), "+v"(dst));
    STAMP(2);
    wait_flag<2>(done);
    STAMP(3);
    u64 x[4];
#pragma nounroll
    for (;;) {
      unsigned fresh = 1;
#pragma unroll
      for (int k = 0; k < 4; ++k) x[k] = __hip_atomic_load(src + k * 256, RLX_AGENT);
#pragma unroll
      for (int k = 0; k < 4; ++k) fresh &= (unsigned)x[k] ^ entry[k];
      if (__all(fresh) || ++spins >= 64 * SPIN_CAP) break;
    }
    STAMP(4);
    float o0 = 0.f, o1 = 0.f;
#pragma unroll
    for (int k = 0; k < 4; ++k) {
      o0 += wk[k] * __uint_as_float((unsigned)x[k] & ~1u);
      o1 += wk[k] * __uint_as_float((unsigned)(x[k] >> 32));
    }
    const float h = fold32(o0, o1);
    const float g = fold16(h, h);
    const auto sw = __builtin_amdgcn_permlane32_swap(__float_as_uint(g), __float_as_uint(g), false, false);
    const unsigned lo = __builtin_bit_cast(unsigned short, (__bf16)__uint_as_float(sw[0]));
    const unsigned hi = __builtin_bit_cast(unsigned short, (__bf16)__uint_as_float(sw[1]));
    if (r == 0) *(__attribute__((address_space(1))) unsigned*)dst = lo | (hi << 16);
    return;
  }

  Tile kr[2][HG], vr[RING];
  Rope rope[2];
  const rsrc_t rk = make_rsrc(qk, KV_BYTES);
  kr[0][0].lo = ld16<2>(rk, kv_voff, first);
  kr[0][0].hi = ld16<2>(rk, kv_voff + 256u, first);
  __builtin_amdgcn_sched_barrier(0);
  const unsigned t0 = (unsigned)__builtin_amdgcn_s_memrealtime();
  __hip_atomic_store(eff_w + wv, t0, RLX_WG);
  STAMP(8);

  const rsrc_t rv = make_rsrc(qv, KV_BYTES);
  const rsrc_t rc = make_rsrc(kcos, CS_BYTES), rs = make_rsrc(ksin, CS_BYTES);
  const unsigned vmask = (((b << 2) ^ (b << 1)) & 16) | ((b >> 4) & 4);
  const unsigned myhead = head_of(wv);
  const unsigned cs_voff = r * 256u + c * 8u;

  const unsigned j = b >> 3;
  const unsigned phk = (j & 15) ^ (j >> 4), phv = (j & 7) ^ ((j >> 1) & 12);
  unsigned due = phk * (PACE_K / 16);
  unsigned org = t0;
  int late = 0;
  auto pace = [&](unsigned per) {
    __builtin_amdgcn_sched_barrier(0);
    due += per;
    const int prev = late;
    late = wait_until(org + (due >> 8));
    if (JUMP) {
      const int jump = late - prev > JUMP ? late - prev : 0;
      org += jump;
      late -= jump;
    }
  };
  auto sync = [&]() {
    const unsigned x = __builtin_amdgcn_readfirstlane(__hip_atomic_load(org_w, RLX_WG));
    org = EARLY_ORG ? ((x - org < 400u) ? x : org) : ((x - t0 + 100u < 500u) ? x : org);
  };
  auto load_k = [&](Tile& d, unsigned t, unsigned u) {
    const unsigned o = (head_of(u) * SKV + key0 + global_key(ktile_key(t) | wkey)) * 512u;
    d.lo = ld16<2>(rk, kv_voff, o);
    d.hi = ld16<2>(rk, kv_voff + 256u, o);
    __builtin_amdgcn_sched_barrier(0);
  };
  auto load_rope = [&](Rope& d, unsigned t) {
    const unsigned o = (key0 + global_key(ktile_key(t) | wkey)) * 256u;
    d.c0 = ld8<0>(rc, cs_voff, o);
    d.c1 = ld8<0>(rc, cs_voff + 128u, o);
    d.s0 = ld8<0>(rs, cs_voff, o);
    d.s1 = ld8<0>(rs, cs_voff + 128u, o);
    __builtin_amdgcn_sched_barrier(0);
  };
  auto load_v = [&](Tile& d, unsigned i) {
    const unsigned o = (myhead * SKV + key0 + global_key(vstep_key(i) ^ vmask)) * 512u;
    d.lo = ld16<2>(rv, kv_voff, o);
    d.hi = ld16<2>(rv, kv_voff + 256u, o);
    __builtin_amdgcn_sched_barrier(0);
  };

  const u32x4 qx = ld16<0>(make_rsrc(q, NH * DH * 2), head_of(r) * (DH * 2u) + c * 16u, 0);
  const u32x4 qc = ld16<0>(make_rsrc(qcos, DH * 2), c * 16u, 0);
  const u32x4 qs = ld16<0>(make_rsrc(qsin, DH * 2), c * 16u, 0);
  const unsigned po = (key0 + global_key((b >> 4) * 32 + wv * 8)) * 256u;
  pace(PACE_P);
  const u32x4 pf0 = ld16<0>(rc, lane * 16u, po), pf1 = ld16<0>(rc, lane * 16u + 1024u, po);
  pace(PACE_P);
  const u32x4 pf2 = ld16<0>(rs, lane * 16u, po), pf3 = ld16<0>(rs, lane * 16u + 1024u, po);
  const unsigned sc_voff = global_key(lane * 8) * 2u, sc_off = (myhead * SKV + key0) * 2u;
  pace(PACE_P);
  const u32x4 rks = ld16<2>(make_rsrc(ksc, SC_BYTES), sc_voff, sc_off);
  const u32x4 rkz = ld16<2>(make_rsrc(kzp, SC_BYTES), sc_voff, sc_off);
  sync();
  pace(PACE_P);
  const u32x4 rvs = ld16<2>(make_rsrc(vsc, SC_BYTES), sc_voff, sc_off);
  const u32x4 rvz = ld16<2>(make_rsrc(vzp, SC_BYTES), sc_voff, sc_off);
  STAMP(9);

  f32x4 qr0[HG], qr1[HG];
  f32x4* lq = reinterpret_cast<f32x4*>(lds + HG * KEYS + wv * (HG * DH / 2));
  gu64* slot = area + W_O + wv * (NSLICE * 64) + slice * 64 + lane;
  unsigned entry;
#pragma unroll
  for (int n = 1; n < RING; ++n) {
    pace(PACE_P);
    load_k(kr[n / HG][n % HG], n / HG, n % HG);
    if (n == 2) entry = (unsigned)__hip_atomic_load(slot, RLX_AGENT);
    if (n % 2 == 1) sync();
    if (n % HG == 1) load_rope(rope[n / HG], n / HG);
    if (n == 4 || n == 5) {
      const int h = n - 4;
      const unsigned sgn = c < 8 ? 0x80008000u : 0u;
      const u32x2 own = {qx[2 * h], qx[2 * h + 1]};
      const u32x2 rot = {(unsigned)__builtin_amdgcn_mov_dpp((int)own[0], 0x128, 0xf, 0xf, true) ^ sgn,
                         (unsigned)__builtin_amdgcn_mov_dpp((int)own[1], 0x128, 0xf, 0xf, true) ^ sgn};
      lq[r * 32 + 2 * c + h] = rbf4(rbf4(unpack4(own) * unpack4(u32x2{qc[2 * h], qc[2 * h + 1]})) +
                                    rbf4(unpack4(rot) * unpack4(u32x2{qs[2 * h], qs[2 * h + 1]})));
      asm volatile("" ::: "memory");
    }
    if (n == 6) {
#pragma unroll
      for (int u = 0; u < HG; ++u) {
        qr0[u] = lq[u * 32 + c];
        qr1[u] = lq[u * 32 + 16 + c];
        asm volatile("" : "+v"(qr0[u]), "+v"(qr1[u]));
      }
      asm volatile("" ::"v"(pf0), "v"(pf1), "v"(pf2), "v"(pf3));
    }
  }
  STAMP(1);

  float* ldk = reinterpret_cast<float*>(lds + wkey + r) + (c & 1);
  f32x4 c0, c1, s0, s1;
  auto unpack_rope = [&](const Rope& d) {
    c0 = unpack4(d.c0); c1 = unpack4(d.c1); s0 = unpack4(d.s0); s1 = unpack4(d.s1);
    asm volatile("" : "+v"(c0), "+v"(c1), "+v"(s0), "+v"(s1));
  };
  auto consume_k = [&](const Tile& d, unsigned t, int u) {
    const f32x4 w0 = qr0[u] * c0 + qr1[u] * s1;
    const f32x4 w1 = qr1[u] * c1 - qr0[u] * s0;
    const f32x4 dv = cvt4(d.lo) * w0 + cvt4(d.hi) * w1, ev = w0 + w1;
    const float dd = (dv[0] + dv[2]) + (dv[1] + dv[3]), ee = (ev[0] + ev[2]) + (ev[1] + ev[3]);
    float x = ((c & 1) ? ee : dd) + dpp<0xB1>((c & 1) ? dd : ee);
    x += dpp<0x4E>(x);
    x += dpp<0x124>(x);
    x += dpp<0x128>(x);
    ldk[2 * (u * KEYS + ktile_key(t))] = x;
  };
  constexpr unsigned KT = STEPS / HG;
  for (unsigned t = 0; t < KT - 2; t += 2) {
    STAMP(t == 2 ? 12 : t == 4 ? 13 : t == 8 ? 14 : t == 16 ? 15 : 63);
#pragma unroll
    for (int h = 0; h < 2; ++h) {
      unpack_rope(rope[h]);
#pragma unroll
      for (int u = 0; u < HG; ++u) {
        consume_k(kr[h][u], t + h, u);
        pace(PACE_K);
        load_k(kr[h][u], t + h + 2, u);
        if (u == 1) load_rope(rope[h], t + h + 2);
        if (u == 2 && h == 0) sync();
      }
    }
  }
  due += phv * (PACE_V / 16) - phk * (PACE_K / 16);
#pragma unroll
  for (int n = 0; n < RING; ++n) {
    if (n % HG == 0) unpack_rope(rope[n / HG]);
    consume_k(kr[n / HG][n % HG], KT - 2 + n / HG, n % HG);
    if (n == 0 || n == 2 || n == 4 || n == 5 || n == 7) {
      const int i = n == 0 ? 0 : n == 2 ? 1 : n == 4 ? 2 : n == 5 ? 3 : 4;
      pace(PACE_V);
      load_v(vr[i], i);
    }
  }
  STAMP(2);
  asm volatile("s_waitcnt lgkmcnt(0)" ::: "memory");
  __builtin_amdgcn_s_barrier();
  asm volatile("" ::: "memory");

  {
    f32x4* pk = reinterpret_cast<f32x4*>(lds + wv * KEYS + lane * 8);
    float s[8];
#pragma unroll
    for (int k = 0; k < 4; ++k) {
      const f32x4 de = pk[k];
      s[2 * k] = (de[0] - bf_lo(rkz[k]) * de[1]) * (bf_lo(rks[k]) * LOGIT_SCALE);
      s[2 * k + 1] = (de[2] - bf_hi(rkz[k]) * de[3]) * (bf_hi(rks[k]) * LOGIT_SCALE);
    }
    float m = s[0];
#pragma unroll
    for (int k = 1; k < 8; ++k) m = fmaxf(m, s[k]);
    asm volatile("" : "+v"(m));
    pace(PACE_V);
    load_v(vr[5], 5);
    m = xrow_max(row_max(m));
    float l = 0.f;
#pragma unroll
    for (int k = 0; k < 4; ++k) {
      const float p0 = __builtin_amdgcn_exp2f(s[2 * k] - m), p1 = __builtin_amdgcn_exp2f(s[2 * k + 1] - m);
      l += p0 + p1;
      pk[k] = f32x4{p0 * bf_lo(rvs[k]), bf_lo(rvz[k]), p1 * bf_hi(rvs[k]), bf_hi(rvz[k])};
    }
    asm volatile("" : "+v"(l)::"memory");
    pace(PACE_V);
    load_v(vr[6], 6);
    l = row_sum(l);
    l = fold16(l, l);
    l = fold32(l, l);
    if (lane == 0) __hip_atomic_store(area + W_ML + wv * NSLICE + slice, pack_pair(m, l), RLX_AGENT);
    pace(PACE_V);
    load_v(vr[7], 7);
  }
  STAMP(3);

  const f32x2* lv = lds + wv * KEYS + r;
  f32x4 a0 = {0.f, 0.f, 0.f, 0.f}, a1 = {0.f, 0.f, 0.f, 0.f};
  auto consume_v = [&](const Tile& d, unsigned i) {
    const f32x2 sc = lv[vstep_key(i) ^ vmask];
    a0 += (cvt4(d.lo) - sc[1]) * sc[0];
    a1 += (cvt4(d.hi) - sc[1]) * sc[0];
    asm volatile("" : "+v"(a0), "+v"(a1));
  };
  for (unsigned i = 0; i < STEPS - RING; i += RING) {
#pragma unroll
    for (int n = 0; n < RING; ++n) {
      consume_v(vr[n], i + n);
      pace(PACE_V);
      load_v(vr[n], i + n + RING);
    }
  }
#pragma unroll
  for (int n = 0; n < RING; ++n) {
    if (n == RING - 1) set_flag(done);
    consume_v(vr[n], STEPS - RING + n);
  }
  STAMP(4);

  const float x0 = fold32(a0[0], a1[0]), x1 = fold32(a0[1], a1[1]);
  const float x2 = fold32(a0[2], a1[2]), x3 = fold32(a0[3], a1[3]);
  const unsigned y0 = (__float_as_uint(fold16(x0, x2)) & ~1u) | (~entry & 1u), y1 = __float_as_uint(fold16(x1, x3));
  __hip_atomic_store(slot, (u64)y0 | ((u64)y1 << 32), RLX_AGENT);
}

}

extern "C" void kernel_launch(void* const* d_in, const int* in_sizes, int n_in, void* d_out, int out_size,
                              void* d_ws, size_t ws_size, hipStream_t stream) {
  using us = const unsigned short*;
  attn_fused<<<NH / HG * NSLICE, (WAVES + 1) * 64, 0, stream>>>(
      (us)d_in[0], (const int*)d_in[1], (us)d_in[2], (us)d_in[3], (const int*)d_in[4], (us)d_in[5], (us)d_in[6],
      (us)d_in[7], (us)d_in[8], (us)d_in[9], (us)d_in[10], static_cast<u64*>(d_ws), static_cast<unsigned*>(d_out));
}
```
